```python
import torch

def _softmax(x, axis=-1):
    return torch.softmax(x, dim=axis)
def _gelu(x):
    return torch.nn.functional.gelu(x, approximate="tanh")
def _silu(x):
    return torch.nn.functional.silu(x)
def _sigmoid(x):
    return torch.sigmoid(x)
def _layernorm(x, axis=-1, eps=1e-5):
    m = x.mean(dim=axis, keepdim=True)
    v = x.var(dim=axis, keepdim=True, unbiased=False)
    return (x - m) / torch.sqrt(v + eps)
def _scatter_add(tgt, idx, src, axis=0):
    return tgt.clone().index_add_(axis, idx, src)
def _segment_sum(src, seg, n):
    out = torch.zeros((n,) + src.shape[1:], dtype=src.dtype)
    return out.index_add_(0, seg, src)
def _pad(x, pw):
    flat = tuple(v for pair in reversed(pw) for v in pair)
    return torch.nn.functional.pad(x, flat)

kernel_name = 'standalone__rezero__sampling'

def setup_inputs():

    _w = (lambda a: (lambda t: t.to(torch.bfloat16) if t.is_floating_point() else t)(torch.as_tensor(a)))
    B, S, D = 1024, 32, 576
    return {
        "x": _w((lambda *s: torch.randn(*s, dtype=torch.bfloat16) * 0.05)(B, S, D)),
        "fx": _w((lambda *s: torch.randn(*s, dtype=torch.bfloat16) * 0.8)(B, S, D)),
        "alpha": _w((lambda *s: torch.randn(*s, dtype=torch.bfloat16) * 0.8)(1) * 0.1),
    }

def reference(x, fx, alpha):
    return x + alpha * fx
```

```cpp
#include <hip/hip_runtime.h>
#include <hip/hip_bf16.h>
#include <stdint.h>

typedef unsigned int u32x4 __attribute__((ext_vector_type(4)));
typedef float f32x2 __attribute__((ext_vector_type(2)));
typedef __bf16 bf16x2 __attribute__((ext_vector_type(2)));

__device__ __forceinline__ float lo_f(unsigned v) { return __builtin_bit_cast(float, v << 16); }
__device__ __forceinline__ float hi_f(unsigned v) { return __builtin_bit_cast(float, v & 0xffff0000u); }
__device__ __forceinline__ unsigned pack_bf16(float a, float b) {
    f32x2 f = {a, b};
    bf16x2 r = __builtin_convertvector(f, bf16x2);
    return __builtin_bit_cast(unsigned, r);
}
__device__ __forceinline__ unsigned rezero2(unsigned x, unsigned f, float a) {
    unsigned t = pack_bf16(a * lo_f(f), a * hi_f(f));
    return pack_bf16(lo_f(x) + lo_f(t), hi_f(x) + hi_f(t));
}

__global__ void __launch_bounds__(256) rezero_kernel(const u32x4* __restrict__ x, const u32x4* __restrict__ fx,
                                                     const unsigned short* __restrict__ alpha,
                                                     u32x4* __restrict__ out, int nvec) {
    int i = blockIdx.x * 256 + threadIdx.x;
    if (i >= nvec) return;
    float a = __builtin_bit_cast(float, (unsigned)alpha[0] << 16);
    u32x4 vx = x[i];
    u32x4 vf = fx[i];
    u32x4 o;
    o.x = rezero2(vx.x, vf.x, a);
    o.y = rezero2(vx.y, vf.y, a);
    o.z = rezero2(vx.z, vf.z, a);
    o.w = rezero2(vx.w, vf.w, a);
    out[i] = o;
}

__global__ void rezero_tail(const unsigned short* __restrict__ x, const unsigned short* __restrict__ fx,
                            const unsigned short* __restrict__ alpha, unsigned short* __restrict__ out,
                            int start, int n) {
    int i = start + threadIdx.x;
    if (i >= n) return;
    float a = __builtin_bit_cast(float, (unsigned)alpha[0] << 16);
    out[i] = (unsigned short)(rezero2(x[i], fx[i], a) & 0xffffu);
}

extern "C" void kernel_launch(void* const* d_in, const int* in_sizes, int n_in,
                              void* d_out, int out_size, void* d_ws, size_t ws_size,
                              hipStream_t stream) {
    const int n = out_size;
    const int nvec = n / 8;
    if (nvec > 0) {
        rezero_kernel<<<(nvec + 255) / 256, 256, 0, stream>>>(
            (const u32x4*)d_in[0], (const u32x4*)d_in[1], (const unsigned short*)d_in[2], (u32x4*)d_out, nvec);
    }
    if (n % 8) {
        rezero_tail<<<1, 64, 0, stream>>>((const unsigned short*)d_in[0], (const unsigned short*)d_in[1],
                                          (const unsigned short*)d_in[2], (unsigned short*)d_out, nvec * 8, n);
    }
}
```

```cpp
#include <hip/hip_runtime.h>
#include <hip/hip_bf16.h>
#include <stdint.h>


typedef unsigned int u32x4 __attribute__((ext_vector_type(4)));
typedef unsigned int u32x16 __attribute__((ext_vector_type(16)));
typedef float f32x2 __attribute__((ext_vector_type(2)));
typedef __bf16 bf16x2 __attribute__((ext_vector_type(2)));
typedef __attribute__((address_space(1))) unsigned int gu32;
typedef __attribute__((address_space(1))) unsigned long long gu64;

extern "C" __device__ unsigned long long rz_dispatch_id() __asm__("llvm.amdgcn.dispatch.id");

constexpr unsigned kBlk = 128;
constexpr unsigned kGroupBytes = 4u << 20;
constexpr unsigned kGroupBlocks = kGroupBytes / (kBlk * 16);
constexpr unsigned kHeadGroups = 3;
constexpr unsigned kNba = 5;
constexpr unsigned kWords = 3 * kNba;
constexpr unsigned kLines = 32;
constexpr unsigned kThr = 112;
constexpr unsigned kTagLo = 0xC35A7000u;
constexpr unsigned kSlotBytes = 128u << 10;
constexpr unsigned kMaxGroups = 256;
static_assert(kWords <= 16, "a replica is 128 bytes");

__device__ __forceinline__ float lo_f(unsigned v) { return __builtin_bit_cast(float, v << 16); }
__device__ __forceinline__ float hi_f(unsigned v) { return __builtin_bit_cast(float, v & 0xffff0000u); }
__device__ __forceinline__ unsigned pack_bf16(float a, float b) {
    f32x2 f = {a, b};
    bf16x2 r = __builtin_convertvector(f, bf16x2);
    return __builtin_bit_cast(unsigned, r);
}
__device__ __forceinline__ unsigned rezero2(unsigned x, unsigned f, float a) {
    const unsigned t = pack_bf16(a * lo_f(f), a * hi_f(f));
    return pack_bf16(lo_f(x) + lo_f(t), hi_f(x) + hi_f(t));
}
__device__ __forceinline__ u32x4 rezero8(u32x4 vx, u32x4 vf, float a) {
    u32x4 o;
    o.x = rezero2(vx.x, vf.x, a);
    o.y = rezero2(vx.y, vf.y, a);
    o.z = rezero2(vx.z, vf.z, a);
    o.w = rezero2(vx.w, vf.w, a);
    return o;
}
__device__ __forceinline__ u32x4 load16(const void* base, unsigned off) {
    u32x4 r;
    asm volatile("global_load_dwordx4 %0, %1, %2 nt" : "=v"(r) : "v"(off), "s"(base) : "memory");
    return r;
}
__device__ __forceinline__ void store16(void* base, unsigned off, u32x4 v) {
    asm volatile("global_store_dwordx4 %0, %1, %2 sc1\n\ts_nop 1" ::"v"(off), "v"(v), "s"(base) : "memory");
}
__device__ __forceinline__ void wait_loads(u32x4& a, u32x4& b) {
    asm volatile("s_waitcnt vmcnt(0)" : "+v"(a), "+v"(b)::"memory");
}

__global__ void __launch_bounds__(kBlk) rezero_main(const void* x, const void* fx, const unsigned* alpha_w, void* out, char* ws,
                                                    unsigned nvote, unsigned head_blocks) {
    __shared__ int s_vote[2];
    unsigned b = blockIdx.x;
    asm volatile("" : "+s"(x), "+s"(fx), "+s"(alpha_w), "+s"(out), "+s"(ws), "+s"(nvote), "+s"(head_blocks));
    const unsigned nprobe = (nvote + 7u) & ~7u;
    const unsigned tag = (unsigned)rz_dispatch_id() * 0x9E3779B1u +
                         (unsigned)(uintptr_t)__builtin_amdgcn_kernarg_segment_ptr() * 0x85EBCA6Bu + 0x7F4A7C15u;
    char* table = ws + (size_t)(tag >> 24) * kSlotBytes;

    if (__builtin_expect(b < nprobe, 0)) {
        if (b >= nvote) return;
        const unsigned grp = head_blocks / kGroupBlocks + b / kWords, bi = b % kWords;
        const unsigned w = __builtin_amdgcn_readfirstlane(threadIdx.x >> 6), lane = threadIdx.x & 63;
        const unsigned arr = bi / kNba, k = bi % kNba, pg = (k + arr) & 1;
        const char* base = arr == 0 ? (const char*)x : arr == 1 ? (const char*)fx : (const char*)out;
        const unsigned unit = ((tag >> 8) + k * 24u + w * 12u) & 127u;
        const unsigned off = (grp * 2 + pg) * 0x200000u + unit * 0x4000u + (lane & (kLines - 1)) * (8192u / kLines);
        const char* pa = base + off + (w ? 8192 : 0);
        const char* pb = base + off + (w ? 0 : 8192);
        unsigned long long t1, t2;
        unsigned va, vb;
        asm volatile("global_load_dword %2, %4, off nt\n\tglobal_load_dword %3, %5, off nt\n\t"
                     "s_waitcnt vmcnt(1)\n\ts_memtime %0\n\ts_waitcnt vmcnt(0)\n\ts_memtime %1\n\ts_waitcnt lgkmcnt(0)"
                     : "=&s"(t1), "=&s"(t2), "=&v"(va), "=&v"(vb) : "v"(pa), "v"(pb) : "memory");
        const unsigned xcc = (unsigned)__builtin_amdgcn_s_getreg(20 | (3 << 11)) & 7u;
        int vote = 0;
        if ((unsigned)(t2 - t1) > kThr) vote = ((w ^ (xcc >> 2) ^ pg) & 1u) ? -1 : 1;
        if (lane == 0) s_vote[w] = vote;
        __syncthreads();
        if (threadIdx.x < 4) {
            unsigned payload = (unsigned)(s_vote[0] + s_vote[1] + 1);
            if (bi == 0) payload |= ((xcc - blockIdx.x) & 7u) << 8;
            __hip_atomic_store((gu64*)(table + grp * 512u + threadIdx.x * 128u + bi * 8u),
                               ((unsigned long long)tag << 32) | kTagLo | payload, __ATOMIC_RELAXED, __HIP_MEMORY_SCOPE_AGENT);
        }
        return;
    }
    b -= nprobe;
    unsigned aw;
    if (b < head_blocks) {
        const unsigned j = b >> 3;
        const unsigned off = ((j >> 8) * kGroupBlocks + (b & 7) * 256u + (j & 255)) * (kBlk * 16) + threadIdx.x * 16u;
        u32x4 vf = load16(fx, off), vx = load16(x, off);
        asm volatile("s_load_dword %0, %1, 0x0\n\ts_waitcnt lgkmcnt(0)" : "=s"(aw) : "s"(alpha_w) : "memory");
        wait_loads(vx, vf);
        store16(out, off, rezero8(vx, vf, __builtin_bit_cast(float, aw << 16)));
        return;
    }
    b -= head_blocks;
    const unsigned r = b & 7, j = b >> 3, jj = j >> 2;
    const unsigned p = (jj >> 6) * 256u + (r & 3) * 64u + (jj & 63);
    const unsigned o0 = (head_blocks / kGroupBlocks) * kGroupBytes + p * 16384u + (j & 3) * 2048u;
    const unsigned page = o0 >> 21;
    const char* line = table + (page >> 1) * 512u + ((j >> 6) & 3u) * 128u;
    u32x16 q0, q1;
    asm volatile("s_load_dword %0, %3, 0x0\n\ts_load_dwordx16 %1, %4, 0x0\n\ts_load_dwordx16 %2, %4, 0x40\n\ts_waitcnt lgkmcnt(0)"
                 : "=&s"(aw), "=&s"(q0), "=&s"(q1) : "s"(alpha_w), "s"(line) : "memory");
    unsigned sh = 0, sa[3] = {0, 0, 0};
#pragma unroll
    for (int i = 0; i < (int)kWords; ++i) {
        sa[i / kNba] += i < 8 ? q0[2 * i] : q1[2 * (i - 8)];
        sh += i < 8 ? q0[2 * i + 1] : q1[2 * (i - 8) + 1];
    }
    unsigned yx = sa[0] - kNba * kTagLo, yf = sa[1] - kNba * kTagLo, yo = sa[2] - kNba * kTagLo;
    bool valid = sh == kWords * tag && (yx & ~0x7FFu) == 0 && yf <= 2 * kNba && yo <= 2 * kNba;
    if (__builtin_expect(!valid, 0)) {
        const unsigned* lw = (const unsigned*)line + (threadIdx.x & 31);
        for (unsigned spins = 0; spins < 512u && !valid; ++spins) {
            const unsigned v = __hip_atomic_load((gu32*)lw, __ATOMIC_RELAXED, __HIP_MEMORY_SCOPE_AGENT);
            sh = 0; sa[0] = sa[1] = sa[2] = 0;
#pragma unroll
            for (int i = 0; i < (int)kWords; ++i) {
                sa[i / kNba] += __builtin_amdgcn_readlane(v, 2 * i);
                sh += __builtin_amdgcn_readlane(v, 2 * i + 1);
            }
            yx = sa[0] - kNba * kTagLo; yf = sa[1] - kNba * kTagLo; yo = sa[2] - kNba * kTagLo;
            valid = sh == kWords * tag && (yx & ~0x7FFu) == 0 && yf <= 2 * kNba && yo <= 2 * kNba;
            if (!valid) __builtin_amdgcn_s_sleep(2);
        }
    }
    const unsigned rot = yx >> 8;
    const int sx = (int)(yx & 0xFFu) - (int)kNba, sf = (int)yf - (int)kNba, so = (int)yo - (int)kNba;
    const int m = (sx > 0) - (sx < 0) + (sf > 0) - (sf < 0) + (so > 0) - (so < 0);
    const unsigned d = ((m < 0 || (m == 0 && sx + sf + so < 0)) ? 1u : 0u) ^ (page & 1u);
    const unsigned cls = d ^ ((((r & 3) + rot) >> 2) & 1u) ^ (r >> 2);
    const unsigned c0 = valid ? cls : 0u, c1 = valid ? cls : 1u;
    const float a = __builtin_bit_cast(float, aw << 16);
    for (unsigned c = c0; c <= c1; ++c) {
        const unsigned off = o0 + c * 8192u + threadIdx.x * 16u;
        u32x4 vf = load16(fx, off), vx = load16(x, off);
        wait_loads(vx, vf);
        store16(out, off, rezero8(vx, vf, a));
    }
}

__global__ void __launch_bounds__(256) rezero_rest(const unsigned short* __restrict__ x, const unsigned short* __restrict__ fx,
                                                   const unsigned short* __restrict__ alpha, unsigned short* __restrict__ out,
                                                   int start, int n, int stride, int vec_ok) {
    const float a = lo_f(alpha[0]);
    const int tid = blockIdx.x * 256 + threadIdx.x;
    int scalar_from = start;
    if (vec_ok) {
        const int nv = (n - start) >> 3;
        const u32x4* vx = (const u32x4*)(x + start);
        const u32x4* vf = (const u32x4*)(fx + start);
        u32x4* vo = (u32x4*)(out + start);
        for (int i = tid; i < nv; i += stride) vo[i] = rezero8(vx[i], vf[i], a);
        scalar_from = start + (nv << 3);
    }
    for (int i = scalar_from + tid; i < n; i += stride)
        out[i] = (unsigned short)(rezero2(x[i], fx[i], a) & 0xffffu);
}

extern "C" void kernel_launch(void* const* d_in, const int* in_sizes, int n_in,
                              void* d_out, int out_size, void* d_ws, size_t ws_size,
                              hipStream_t stream) {
    const int n = out_size;
    const bool aligned = (((uintptr_t)d_in[0] | (uintptr_t)d_in[1] | (uintptr_t)d_out) & 15) == 0;
    const bool alpha_ok = (((uintptr_t)d_in[2]) & 3) == 0;
    constexpr int kGroupElems = (int)(kGroupBytes / 2);
    const int ngroups = (aligned && alpha_ok) ? n / kGroupElems : 0;
    if (ngroups > 0) {
        const bool paired = ngroups > (int)kHeadGroups && ngroups <= (int)kMaxGroups && ws_size >= (size_t)256 * kSlotBytes &&
                            (((uintptr_t)d_ws) & 127) == 0;
        const unsigned head_groups = paired ? kHeadGroups : (unsigned)ngroups;
        const unsigned nvote = ((unsigned)ngroups - head_groups) * kWords;
        const unsigned grid = ((nvote + 7u) & ~7u) + (unsigned)ngroups * kGroupBlocks;
        rezero_main<<<grid, kBlk, 0, stream>>>(d_in[0], d_in[1], (const unsigned*)d_in[2], d_out, (char*)d_ws, nvote,
                                               head_groups * kGroupBlocks);
    }
    const int done = ngroups * kGroupElems;
    if (done < n) {
        int grid = ((n - done) / 8 + 255) / 256 + 1;
        if (grid > 2048) grid = 2048;
        rezero_rest<<<grid, 256, 0, stream>>>((const unsigned short*)d_in[0], (const unsigned short*)d_in[1],
                                              (const unsigned short*)d_in[2], (unsigned short*)d_out, done, n, grid * 256,
                                              aligned ? 1 : 0);
    }
}
```

```cpp
#include <hip/hip_runtime.h>
#include <hip/hip_bf16.h>
#include <stdint.h>


typedef unsigned int u32x4 __attribute__((ext_vector_type(4)));
typedef float f32x2 __attribute__((ext_vector_type(2)));
typedef __bf16 bf16x2 __attribute__((ext_vector_type(2)));

extern "C" __device__ unsigned long long rz_dispatch_id() __asm__("llvm.amdgcn.dispatch.id");

constexpr unsigned kGroupBytes = 4u << 20;
constexpr unsigned kGroupBlocks = 2048;
constexpr unsigned kHeadGroups = 3;
constexpr unsigned kMaxGroups = 256;
constexpr size_t kTableBytes = (size_t)256 * 131072;

__global__ void __launch_bounds__(128) rezero_vote(const void* x, const void* fx, const void* alpha, void* out, void* ws,
                                                   unsigned ngroups, unsigned unused) {
    const unsigned long long ka = (unsigned long long)(uintptr_t)__builtin_amdgcn_kernarg_segment_ptr();
    asm volatile(
        ".Lrz_ENTRY_%=:\n"
        "\ts_load_dwordx8 s[8:15], %[ka], 0x0\n"
        "\ts_load_dwordx4 s[16:19], %[ka], 0x20\n"
        "\ts_mov_b32 s22, 0\n"
        "\ts_cmpk_lt_u32 %[bid], 104\n"
        "\ts_cbranch_scc1 .Lrz_PROBE_%=\n"
        "\ts_cmpk_ge_u32 %[bid], 6144\n"
        "\ts_cbranch_scc1 .Lrz_PAIRED_%=\n"
        ".Lrz_HEAD_%=:\n"
        "\ts_and_b32 s25, %[bid], 0xfffff800\n"
        "\ts_lshl_b32 s26, %[bid], 8\n"
        "\ts_and_b32 s26, s26, 0x700\n"
        "\ts_or_b32 s25, s25, s26\n"
        "\ts_bfe_u32 s26, %[bid], 0x80003\n"
        "\ts_or_b32 s25, s25, s26\n"
        "\ts_lshl_b32 s25, s25, 11\n"
        "\tv_lshlrev_b32_e32 v1, 4, %[tid]\n"
        "\tv_or_b32_e32 v1, s25, v1\n"
        ".Lrz_LOADS_%=:\n"
        "\ts_waitcnt lgkmcnt(0)\n"
        "\tglobal_load_dwordx4 v[2:5], v1, s[10:11] nt\n"
        "\tglobal_load_dwordx4 v[6:9], v1, s[8:9] nt\n"
        "\ts_load_dword s20, s[12:13], 0x0\n"
        ".Lrz_ISSUED_%=:\n"
        "\ts_mov_b32 s21, 0xffff0000\n"
        "\ts_waitcnt lgkmcnt(0)\n"
        "\ts_lshl_b32 s20, s20, 16\n"
        "\ts_waitcnt vmcnt(1)\n"
        "\tv_lshlrev_b32_e32 v10, 16, v2\n"
        "\tv_and_b32_e32 v11, s21, v2\n"
        "\tv_lshlrev_b32_e32 v12, 16, v3\n"
        "\tv_and_b32_e32 v13, s21, v3\n"
        "\tv_lshlrev_b32_e32 v14, 16, v4\n"
        "\tv_and_b32_e32 v15, s21, v4\n"
        "\tv_lshlrev_b32_e32 v16, 16, v5\n"
        "\tv_and_b32_e32 v17, s21, v5\n"
        "\tv_pk_mul_f32 v[10:11], s[20:21], v[10:11] op_sel_hi:[0,1]\n"
        "\tv_pk_mul_f32 v[12:13], s[20:21], v[12:13] op_sel_hi:[0,1]\n"
        "\tv_pk_mul_f32 v[14:15], s[20:21], v[14:15] op_sel_hi:[0,1]\n"
        "\tv_pk_mul_f32 v[16:17], s[20:21], v[16:17] op_sel_hi:[0,1]\n"
        "\tv_cvt_pk_bf16_f32 v2, v10, v11\n"
        "\tv_cvt_pk_bf16_f32 v3, v12, v13\n"
        "\tv_cvt_pk_bf16_f32 v4, v14, v15\n"
        "\tv_cvt_pk_bf16_f32 v5, v16, v17\n"
        "\tv_lshlrev_b32_e32 v10, 16, v2\n"
        "\tv_and_b32_e32 v11, s21, v2\n"
        "\tv_lshlrev_b32_e32 v12, 16, v3\n"
        "\tv_and_b32_e32 v13, s21, v3\n"
        "\tv_lshlrev_b32_e32 v14, 16, v4\n"
        "\tv_and_b32_e32 v15, s21, v4\n"
        "\tv_lshlrev_b32_e32 v16, 16, v5\n"
        "\tv_and_b32_e32 v17, s21, v5\n"
        "\ts_waitcnt vmcnt(0)\n"
        "\tv_lshlrev_b32_e32 v2, 16, v6\n"
        "\tv_and_b32_e32 v3, s21, v6\n"
        "\tv_lshlrev_b32_e32 v4, 16, v7\n"
        "\tv_and_b32_e32 v5, s21, v7\n"
        "\tv_lshlrev_b32_e32 v6, 16, v8\n"
        "\tv_and_b32_e32 v7, s21, v8\n"
        "\tv_lshlrev_b32_e32 v8, 16, v9\n"
        "\tv_and_b32_e32 v9, s21, v9\n"
        "\tv_pk_add_f32 v[2:3], v[2:3], v[10:11]\n"
        "\tv_pk_add_f32 v[4:5], v[4:5], v[12:13]\n"
        "\tv_pk_add_f32 v[6:7], v[6:7], v[14:15]\n"
        "\tv_pk_add_f32 v[8:9], v[8:9], v[16:17]\n"
        "\tv_cvt_pk_bf16_f32 v2, v2, v3\n"
        "\tv_cvt_pk_bf16_f32 v3, v4, v5\n"
        "\tv_cvt_pk_bf16_f32 v4, v6, v7\n"
        "\tv_cvt_pk_bf16_f32 v5, v8, v9\n"
        "\tglobal_store_dwordx4 v1, v[2:5], s[14:15] sc1\n"
        "\ts_cmp_eq_u32 s22, 0\n"
        "\ts_cbranch_scc1 .Lrz_END_%=\n"
        "\ts_mov_b32 s22, 0\n"
        "\ts_nop 1\n"
        "\tv_xor_b32_e32 v1, 0x2000, v1\n"
        "\ts_branch .Lrz_LOADS_%=\n"
        ".Lrz_PAIRED_%=:\n"
        "\ts_sub_u32 s24, %[bid], 6144\n"
        "\ts_and_b32 s25, s24, 7\n"
        "\ts_lshr_b32 s26, s24, 3\n"
        "\ts_lshr_b32 s27, s24, 11\n"
        "\ts_add_u32 s27, s27, 3\n"
        "\ts_bfe_u32 s28, s24, 0x60005\n"
        "\ts_and_b32 s29, s24, 3\n"
        "\ts_lshl_b32 s30, s29, 6\n"
        "\ts_or_b32 s28, s28, s30\n"
        "\ts_lshl_b32 s28, s28, 14\n"
        "\ts_lshl_b32 s30, s27, 22\n"
        "\ts_or_b32 s28, s28, s30\n"
        "\ts_and_b32 s30, s26, 3\n"
        "\ts_lshl_b32 s30, s30, 11\n"
        "\ts_or_b32 s28, s28, s30\n"
        "\ts_mul_i32 s31, %[did], 0x9e3779b1\n"
        "\ts_mul_i32 s30, %[kalo], 0x85ebca6b\n"
        "\ts_add_u32 s31, s31, s30\n"
        "\ts_add_u32 s31, s31, 0x7f4a7c15\n"
        "\ts_lshr_b32 s66, s31, 24\n"
        "\ts_lshl_b32 s66, s66, 17\n"
        "\ts_lshl_b32 s30, s27, 9\n"
        "\ts_add_u32 s66, s66, s30\n"
        "\ts_bfe_u32 s30, s26, 0x20006\n"
        "\ts_lshl_b32 s30, s30, 7\n"
        "\ts_add_u32 s66, s66, s30\n"
        "\tv_lshlrev_b32_e32 v1, 4, %[tid]\n"
        "\ts_movk_i32 s23, 512\n"
        "\ts_mul_i32 s68, s31, 15\n"
        "\ts_waitcnt lgkmcnt(0)\n"
        "\ts_add_u32 s34, s16, s66\n"
        "\ts_addc_u32 s35, s17, 0\n"
        "\ts_load_dwordx16 s[36:51], s[34:35], 0x0\n"
        "\ts_load_dwordx16 s[52:67], s[34:35], 0x40\n"
        "\ts_waitcnt lgkmcnt(0)\n"
        ".Lrz_SUMS_%=:\n"
        "\ts_add_u32 s66, s37, s39\n"
        "\ts_add_u32 s67, s36, s38\n"
        "\ts_add_u32 s69, s46, s48\n"
        "\ts_add_u32 s70, s56, s58\n"
        "\ts_add_u32 s66, s66, s41\n"
        "\ts_add_u32 s67, s67, s40\n"
        "\ts_add_u32 s69, s69, s50\n"
        "\ts_add_u32 s70, s70, s60\n"
        "\ts_add_u32 s66, s66, s43\n"
        "\ts_add_u32 s67, s67, s42\n"
        "\ts_add_u32 s69, s69, s52\n"
        "\ts_add_u32 s70, s70, s62\n"
        "\ts_add_u32 s66, s66, s45\n"
        "\ts_add_u32 s67, s67, s44\n"
        "\ts_add_u32 s69, s69, s54\n"
        "\ts_add_u32 s70, s70, s64\n"
        "\ts_add_u32 s66, s66, s47\n"
        "\ts_add_u32 s66, s66, s49\n"
        "\ts_add_u32 s66, s66, s51\n"
        "\ts_add_u32 s66, s66, s53\n"
        "\ts_add_u32 s66, s66, s55\n"
        "\ts_add_u32 s66, s66, s57\n"
        "\ts_add_u32 s66, s66, s59\n"
        "\ts_add_u32 s66, s66, s61\n"
        "\ts_add_u32 s66, s66, s63\n"
        "\ts_add_u32 s66, s66, s65\n"
        "\ts_xor_b32 s71, s31, 0xc35a7000\n"
        "\ts_and_b32 s71, s71, 0xfffff000\n"
        "\ts_mul_i32 s71, s71, 5\n"
        "\ts_sub_u32 s67, s67, s71\n"
        "\ts_sub_u32 s69, s69, s71\n"
        "\ts_sub_u32 s70, s70, s71\n"
        "\ts_xor_b32 s66, s66, s68\n"
        "\ts_lshr_b32 s71, s67, 11\n"
        "\ts_or_b32 s66, s66, s71\n"
        "\ts_max_u32 s71, s69, s70\n"
        "\ts_cmp_gt_u32 s71, 10\n"
        "\ts_cselect_b32 s71, 1, 0\n"
        "\ts_or_b32 s66, s66, s71\n"
        "\ts_cbranch_scc1 .Lrz_SLOW_%=\n"
        "\ts_lshr_b32 s71, s67, 8\n"
        "\ts_and_b32 s67, s67, 0xff\n"
        "\ts_sub_i32 s67, s67, 5\n"
        "\ts_sub_i32 s69, s69, 5\n"
        "\ts_sub_i32 s70, s70, 5\n"
        "\ts_add_i32 s72, s67, s69\n"
        "\ts_add_i32 s72, s72, s70\n"
        "\ts_max_i32 s67, s67, -1\n"
        "\ts_min_i32 s67, s67, 1\n"
        "\ts_max_i32 s69, s69, -1\n"
        "\ts_min_i32 s69, s69, 1\n"
        "\ts_max_i32 s70, s70, -1\n"
        "\ts_min_i32 s70, s70, 1\n"
        "\ts_add_i32 s67, s67, s69\n"
        "\ts_add_i32 s67, s67, s70\n"
        "\ts_lshl_b32 s67, s67, 6\n"
        "\ts_add_i32 s67, s67, s72\n"
        "\ts_lshr_b32 s67, s67, 31\n"
        "\ts_lshr_b32 s30, s28, 21\n"
        "\ts_xor_b32 s67, s67, s30\n"
        "\ts_add_u32 s71, s71, s29\n"
        "\ts_lshr_b32 s71, s71, 2\n"
        "\ts_xor_b32 s67, s67, s71\n"
        "\ts_lshr_b32 s30, s25, 2\n"
        "\ts_xor_b32 s67, s67, s30\n"
        "\ts_and_b32 s67, s67, 1\n"
        "\ts_lshl_b32 s67, s67, 13\n"
        "\ts_or_b32 s28, s28, s67\n"
        "\tv_or_b32_e32 v1, s28, v1\n"
        "\ts_branch .Lrz_LOADS_%=\n"
        ".Lrz_SLOW_%=:\n"
        "\ts_cmp_eq_u32 s23, 0\n"
        "\ts_cbranch_scc1 .Lrz_GIVEUP_%=\n"
        "\ts_sub_u32 s23, s23, 1\n"
        "\tv_and_b32_e32 v10, 15, %[tid]\n"
        "\tv_lshlrev_b32_e32 v10, 3, v10\n"
        "\ts_sleep 2\n"
        "\tglobal_load_dwordx2 v[12:13], v10, s[34:35] sc1\n"
        "\ts_waitcnt vmcnt(0)\n"
        "\tv_readlane_b32 s36, v12, 0\n"
        "\tv_readlane_b32 s37, v13, 0\n"
        "\tv_readlane_b32 s38, v12, 1\n"
        "\tv_readlane_b32 s39, v13, 1\n"
        "\tv_readlane_b32 s40, v12, 2\n"
        "\tv_readlane_b32 s41, v13, 2\n"
        "\tv_readlane_b32 s42, v12, 3\n"
        "\tv_readlane_b32 s43, v13, 3\n"
        "\tv_readlane_b32 s44, v12, 4\n"
        "\tv_readlane_b32 s45, v13, 4\n"
        "\tv_readlane_b32 s46, v12, 5\n"
        "\tv_readlane_b32 s47, v13, 5\n"
        "\tv_readlane_b32 s48, v12, 6\n"
        "\tv_readlane_b32 s49, v13, 6\n"
        "\tv_readlane_b32 s50, v12, 7\n"
        "\tv_readlane_b32 s51, v13, 7\n"
        "\tv_readlane_b32 s52, v12, 8\n"
        "\tv_readlane_b32 s53, v13, 8\n"
        "\tv_readlane_b32 s54, v12, 9\n"
        "\tv_readlane_b32 s55, v13, 9\n"
        "\tv_readlane_b32 s56, v12, 10\n"
        "\tv_readlane_b32 s57, v13, 10\n"
        "\tv_readlane_b32 s58, v12, 11\n"
        "\tv_readlane_b32 s59, v13, 11\n"
        "\tv_readlane_b32 s60, v12, 12\n"
        "\tv_readlane_b32 s61, v13, 12\n"
        "\tv_readlane_b32 s62, v12, 13\n"
        "\tv_readlane_b32 s63, v13, 13\n"
        "\tv_readlane_b32 s64, v12, 14\n"
        "\tv_readlane_b32 s65, v13, 14\n"
        "\ts_branch .Lrz_SUMS_%=\n"
        ".Lrz_GIVEUP_%=:\n"
        "\ts_mov_b32 s22, 1\n"
        "\tv_or_b32_e32 v1, s28, v1\n"
        "\ts_branch .Lrz_LOADS_%=\n"
        ".Lrz_PROBE_%=:\n"
        "\ts_cmpk_lt_u32 %[bid], 90\n"
        "\ts_cbranch_scc1 .Lrz_PROBE2_%=\n"
        "\ts_getpc_b64 s[34:35]\n"
        ".Lrz_PFN_%=:\n"
        "\ts_sub_u32 s34, s34, .Lrz_PFN_%=-.Lrz_ENTRY_%=\n"
        "\ts_subb_u32 s35, s35, 0\n"
        "\tv_and_b32_e32 v2, 63, %[tid]\n"
        "\tv_lshlrev_b32_e32 v2, 5, v2\n"
        "\tv_min_u32_e32 v2, .Lrz_END_%=-.Lrz_ENTRY_%=, v2\n"
        "\tglobal_load_dword v18, v2, s[34:35]\n"
        "\ts_branch .Lrz_HEAD_%=\n"
        ".Lrz_PROBE2_%=:\n"
        "\ts_mul_i32 s24, %[bid], 4370\n"
        "\ts_lshr_b32 s24, s24, 16\n"
        "\ts_mul_i32 s25, s24, 15\n"
        "\ts_sub_u32 s25, %[bid], s25\n"
        "\ts_mul_i32 s26, s25, 52\n"
        "\ts_lshr_b32 s26, s26, 8\n"
        "\ts_mul_i32 s27, s26, 5\n"
        "\ts_sub_u32 s27, s25, s27\n"
        "\ts_add_u32 s28, s27, s26\n"
        "\ts_and_b32 s28, s28, 1\n"
        "\ts_add_u32 s24, s24, 3\n"
        "\tv_lshrrev_b32_e32 v2, 6, %[tid]\n"
        "\ts_mul_i32 s31, %[did], 0x9e3779b1\n"
        "\ts_mul_i32 s40, %[kalo], 0x85ebca6b\n"
        "\ts_add_u32 s31, s31, s40\n"
        "\ts_add_u32 s31, s31, 0x7f4a7c15\n"
        "\tv_readfirstlane_b32 s29, v2\n"
        "\ts_lshr_b32 s30, s31, 8\n"
        "\ts_mul_i32 s40, s27, 24\n"
        "\ts_add_u32 s30, s30, s40\n"
        "\ts_mul_i32 s40, s29, 12\n"
        "\ts_add_u32 s30, s30, s40\n"
        "\ts_and_b32 s30, s30, 127\n"
        "\ts_lshl_b32 s30, s30, 14\n"
        "\ts_lshl_b32 s40, s29, 13\n"
        "\ts_or_b32 s30, s30, s40\n"
        "\ts_lshl_b32 s40, s28, 21\n"
        "\ts_or_b32 s30, s30, s40\n"
        "\tv_and_b32_e32 v2, 31, %[tid]\n"
        "\tv_lshlrev_b32_e32 v2, 8, v2\n"
        ".Lrz_ROUND_%=:\n"
        "\ts_lshl_b32 s40, s24, 22\n"
        "\ts_or_b32 s40, s40, s30\n"
        "\tv_or_b32_e32 v3, s40, v2\n"
        "\tv_xor_b32_e32 v4, 0x2000, v3\n"
        "\ts_waitcnt lgkmcnt(0)\n"
        "\ts_cmp_ge_u32 s24, s18\n"
        "\ts_cbranch_scc1 .Lrz_HEAD_%=\n"
        "\ts_cmp_eq_u32 s26, 1\n"
        "\ts_cselect_b64 s[34:35], s[10:11], s[14:15]\n"
        "\ts_cmp_eq_u32 s26, 0\n"
        "\ts_cselect_b64 s[34:35], s[8:9], s[34:35]\n"
        "\t.p2align 6\n"
        "\tglobal_load_dword v5, v3, s[34:35] nt\n"
        "\tglobal_load_dword v6, v4, s[34:35] nt\n"
        "\ts_waitcnt vmcnt(1)\n"
        "\ts_memtime s[36:37]\n"
        "\ts_waitcnt vmcnt(0)\n"
        "\ts_memtime s[38:39]\n"
        "\ts_getreg_b32 s42, hwreg(HW_REG_XCC_ID, 0, 3)\n"
        "\ts_lshr_b32 s43, s42, 2\n"
        "\ts_xor_b32 s43, s43, s29\n"
        "\ts_xor_b32 s43, s43, s28\n"
        "\ts_and_b32 s43, s43, 1\n"
        "\ts_lshl_b32 s43, s43, 1\n"
        "\ts_sub_i32 s43, 1, s43\n"
        "\ts_waitcnt lgkmcnt(0)\n"
        "\ts_sub_u32 s44, s38, s36\n"
        "\ts_cmp_gt_u32 s44, 112\n"
        "\ts_cselect_b32 s43, s43, 0\n"
        "\ts_lshl_b32 s44, s29, 2\n"
        "\tv_mov_b32_e32 v7, s43\n"
        "\tv_mov_b32_e32 v8, s44\n"
        "\tds_write_b32 v8, v7\n"
        "\ts_waitcnt lgkmcnt(0)\n"
        "\ts_barrier\n"
        "\ts_cmp_lg_u32 s29, 0\n"
        "\ts_cbranch_scc1 .Lrz_NEXT_%=\n"
        "\tv_mov_b32_e32 v8, 0\n"
        "\tds_read_b64 v[10:11], v8\n"
        "\ts_sub_u32 s44, s42, %[bid]\n"
        "\ts_and_b32 s44, s44, 7\n"
        "\ts_lshl_b32 s44, s44, 8\n"
        "\ts_cmp_eq_u32 s25, 0\n"
        "\ts_cselect_b32 s44, s44, 0\n"
        "\ts_xor_b32 s45, s31, 0xc35a7000\n"
        "\ts_and_b32 s45, s45, 0xfffff000\n"
        "\ts_or_b32 s44, s44, s45\n"
        "\ts_lshr_b32 s45, s31, 24\n"
        "\ts_lshl_b32 s45, s45, 17\n"
        "\ts_lshl_b32 s46, s24, 9\n"
        "\ts_add_u32 s45, s45, s46\n"
        "\ts_lshl_b32 s46, s25, 3\n"
        "\ts_add_u32 s45, s45, s46\n"
        "\tv_lshlrev_b32_e32 v12, 7, %[tid]\n"
        "\tv_add_u32_e32 v12, s45, v12\n"
        "\ts_waitcnt lgkmcnt(0)\n"
        "\tv_add_u32_e32 v10, v10, v11\n"
        "\tv_add_u32_e32 v10, 1, v10\n"
        "\tv_or_b32_e32 v10, s44, v10\n"
        "\tv_mov_b32_e32 v11, s31\n"
        "\ts_mov_b64 exec, 15\n"
        "\tglobal_store_dwordx2 v12, v[10:11], s[16:17] sc1\n"
        "\ts_mov_b64 exec, -1\n"
        ".Lrz_NEXT_%=:\n"
        "\ts_add_u32 s24, s24, 6\n"
        "\ts_cmp_ge_u32 s24, s18\n"
        "\ts_cbranch_scc1 .Lrz_HEAD_%=\n"
        "\ts_barrier\n"
        "\ts_branch .Lrz_ROUND_%=\n"
        ".Lrz_END_%=:\n"
        :
        : [ka] "s"(ka), [kalo] "s"((unsigned)ka), [did] "s"((unsigned)rz_dispatch_id()), [bid] "s"(blockIdx.x),
          [tid] "v"(threadIdx.x)
        : "memory", "scc", "vcc", "s8", "s9", "s10", "s11", "s12", "s13", "s14", "s15", "s16", "s17", "s18", "s19", "s20", "s21", "s22", "s23", "s24", "s25", "s26", "s27", "s28", "s29", "s30", "s31", "s34", "s35", "s36", "s37", "s38", "s39", "s40", "s41", "s42", "s43", "s44", "s45", "s46", "s47", "s48", "s49", "s50", "s51", "s52", "s53", "s54", "s55", "s56", "s57", "s58", "s59", "s60", "s61", "s62", "s63", "s64", "s65", "s66", "s67", "s68", "s69", "s70", "s71", "s72", "v1", "v2", "v3", "v4", "v5", "v6", "v7", "v8", "v9", "v10", "v11", "v12", "v13", "v14", "v15", "v16", "v17", "v18");
}

__device__ __forceinline__ float lo_f(unsigned v) { return __builtin_bit_cast(float, v << 16); }
__device__ __forceinline__ float hi_f(unsigned v) { return __builtin_bit_cast(float, v & 0xffff0000u); }
__device__ __forceinline__ unsigned pack_bf16(float a, float b) {
    f32x2 f = {a, b};
    bf16x2 r = __builtin_convertvector(f, bf16x2);
    return __builtin_bit_cast(unsigned, r);
}
__device__ __forceinline__ unsigned rezero2(unsigned x, unsigned f, float a) {
    const unsigned t = pack_bf16(a * lo_f(f), a * hi_f(f));
    return pack_bf16(lo_f(x) + lo_f(t), hi_f(x) + hi_f(t));
}
__device__ __forceinline__ u32x4 rezero8(u32x4 vx, u32x4 vf, float a) {
    u32x4 o;
    o.x = rezero2(vx.x, vf.x, a);
    o.y = rezero2(vx.y, vf.y, a);
    o.z = rezero2(vx.z, vf.z, a);
    o.w = rezero2(vx.w, vf.w, a);
    return o;
}

__global__ void __launch_bounds__(256) rezero_rest(const unsigned short* __restrict__ x, const unsigned short* __restrict__ fx,
                                                   const unsigned short* __restrict__ alpha, unsigned short* __restrict__ out,
                                                   int start, int n, int stride, int vec_ok) {
    const float a = lo_f(alpha[0]);
    const int tid = blockIdx.x * 256 + threadIdx.x;
    int scalar_from = start;
    if (vec_ok) {
        const int nv = (n - start) >> 3;
        const u32x4* vx = (const u32x4*)(x + start);
        const u32x4* vf = (const u32x4*)(fx + start);
        u32x4* vo = (u32x4*)(out + start);
        for (int i = tid; i < nv; i += stride)
            __builtin_nontemporal_store(rezero8(__builtin_nontemporal_load(vx + i), __builtin_nontemporal_load(vf + i), a), vo + i);
        scalar_from = start + (nv << 3);
    }
    for (int i = scalar_from + tid; i < n; i += stride)
        out[i] = (unsigned short)(rezero2(x[i], fx[i], a) & 0xffffu);
}

extern "C" void kernel_launch(void* const* d_in, const int* in_sizes, int n_in,
                              void* d_out, int out_size, void* d_ws, size_t ws_size,
                              hipStream_t stream) {
    const int n = out_size;
    const bool aligned = (((uintptr_t)d_in[0] | (uintptr_t)d_in[1] | (uintptr_t)d_out) & 15) == 0;
    const bool alpha_ok = (((uintptr_t)d_in[2]) & 3) == 0;
    constexpr int kGroupElems = (int)(kGroupBytes / 2);
    int ngroups = (aligned && alpha_ok) ? n / kGroupElems : 0;
    if (ngroups <= (int)kHeadGroups || ngroups > (int)kMaxGroups || ws_size < kTableBytes || (((uintptr_t)d_ws) & 127) != 0)
        ngroups = 0;
    if (ngroups > 0)
        rezero_vote<<<(unsigned)ngroups * kGroupBlocks, 128, 16, stream>>>(d_in[0], d_in[1], d_in[2], d_out, d_ws, (unsigned)ngroups, 0u);
    const int done = ngroups * kGroupElems;
    if (done < n) {
        int grid = ((n - done) / 8 + 255) / 256 + 1;
        if (grid > 8192) grid = 8192;
        rezero_rest<<<grid, 256, 0, stream>>>((const unsigned short*)d_in[0], (const unsigned short*)d_in[1],
                                              (const unsigned short*)d_in[2], (unsigned short*)d_out, done, n, grid * 256,
                                              aligned ? 1 : 0);
    }
}
```
